```python
import torch

def _softmax(x, axis=-1):
    return torch.softmax(x, dim=axis)
def _gelu(x):
    return torch.nn.functional.gelu(x, approximate="tanh")
def _silu(x):
    return torch.nn.functional.silu(x)
def _sigmoid(x):
    return torch.sigmoid(x)
def _layernorm(x, axis=-1, eps=1e-5):
    m = x.mean(dim=axis, keepdim=True)
    v = x.var(dim=axis, keepdim=True, unbiased=False)
    return (x - m) / torch.sqrt(v + eps)
def _scatter_add(tgt, idx, src, axis=0):
    return tgt.clone().index_add_(axis, idx, src)
def _segment_sum(src, seg, n):
    out = torch.zeros((n,) + src.shape[1:], dtype=src.dtype)
    return out.index_add_(0, seg, src)
def _pad(x, pw):
    flat = tuple(v for pair in reversed(pw) for v in pair)
    return torch.nn.functional.pad(x, flat)

kernel_name = 'standalone__consistency_rf_step__diffusion'

def setup_inputs():

    _w = (lambda a: (lambda t: t.to(torch.bfloat16) if t.is_floating_point() else t)(torch.as_tensor(a)))
    B, C, HW = 2, 320, 128
    return {
        "x": _w((lambda *s: torch.randn(*s, dtype=torch.bfloat16) * 0.05)(B, C, HW, HW)),
        "v": _w((lambda *s: torch.randn(*s, dtype=torch.bfloat16) * 0.05)(B, C, HW, HW)),
        "sigma": _w(torch.rand(B).to(float)),
    }

def reference(x, v, sigma):
    s = sigma.reshape(-1, 1, 1, 1)
    return x - s * v
```

```cpp
#include <hip/hip_runtime.h>
#include <hip/hip_bf16.h>
#include <stdint.h>

typedef unsigned int u32x4 __attribute__((ext_vector_type(4)));
typedef float f32x2 __attribute__((ext_vector_type(2)));
typedef __bf16 bf16x2 __attribute__((ext_vector_type(2)));

__device__ __forceinline__ unsigned int rf_pair(unsigned int xw, unsigned int vw, float s) {
    f32x2 xf, vf;
    xf.x = __uint_as_float(xw << 16);
    xf.y = __uint_as_float(xw & 0xffff0000u);
    vf.x = __uint_as_float(vw << 16);
    vf.y = __uint_as_float(vw & 0xffff0000u);
    f32x2 p = vf * s;
    bf16x2 tb = __builtin_convertvector(p, bf16x2);
    unsigned int tw = __builtin_bit_cast(unsigned int, tb);
    f32x2 tf;
    tf.x = __uint_as_float(tw << 16);
    tf.y = __uint_as_float(tw & 0xffff0000u);
    f32x2 r = xf - tf;
    bf16x2 rb = __builtin_convertvector(r, bf16x2);
    return __builtin_bit_cast(unsigned int, rb);
}

#define BLOCK 256
#define UNROLL 4

__global__ __launch_bounds__(BLOCK) void rf_step_vec(const u32x4* __restrict__ x, const u32x4* __restrict__ v,
                                                     const unsigned short* __restrict__ sigma,
                                                     u32x4* __restrict__ out, unsigned int blocks_per_batch) {
    const unsigned int b = blockIdx.x / blocks_per_batch;
    const float s = __uint_as_float(((unsigned int)sigma[b]) << 16);
    const size_t base = (size_t)blockIdx.x * (BLOCK * UNROLL) + threadIdx.x;
    u32x4 xa[UNROLL], va[UNROLL];
#pragma unroll
    for (int u = 0; u < UNROLL; ++u) {
        xa[u] = x[base + u * BLOCK];
        va[u] = v[base + u * BLOCK];
    }
#pragma unroll
    for (int u = 0; u < UNROLL; ++u) {
        u32x4 o;
        o.x = rf_pair(xa[u].x, va[u].x, s);
        o.y = rf_pair(xa[u].y, va[u].y, s);
        o.z = rf_pair(xa[u].z, va[u].z, s);
        o.w = rf_pair(xa[u].w, va[u].w, s);
        out[base + u * BLOCK] = o;
    }
}

__global__ void rf_step_scalar(const unsigned short* __restrict__ x, const unsigned short* __restrict__ v,
                               const unsigned short* __restrict__ sigma, unsigned short* __restrict__ out,
                               long long n, long long per_batch) {
    long long i = (long long)blockIdx.x * blockDim.x + threadIdx.x;
    const long long stride = (long long)gridDim.x * blockDim.x;
    for (; i < n; i += stride) {
        const float s = __uint_as_float(((unsigned int)sigma[i / per_batch]) << 16);
        out[i] = (unsigned short)(rf_pair(x[i], v[i], s) & 0xffffu);
    }
}

extern "C" void kernel_launch(void* const* d_in, const int* in_sizes, int n_in,
                              void* d_out, int out_size, void* d_ws, size_t ws_size,
                              hipStream_t stream) {
    const long long n = in_sizes[0];
    const long long nb = in_sizes[2] > 0 ? in_sizes[2] : 1;
    const long long per_batch = n / nb;
    const long long chunk = (long long)BLOCK * UNROLL * 8;
    const bool aligned = (((uintptr_t)d_in[0] | (uintptr_t)d_in[1] | (uintptr_t)d_out) & 15) == 0;
    if (aligned && per_batch > 0 && per_batch % chunk == 0 && per_batch * nb == n) {
        const unsigned int bpb = (unsigned int)(per_batch / chunk);
        rf_step_vec<<<(unsigned int)(bpb * nb), BLOCK, 0, stream>>>(
            (const u32x4*)d_in[0], (const u32x4*)d_in[1], (const unsigned short*)d_in[2], (u32x4*)d_out, bpb);
    } else if (n > 0) {
        long long g = (n + 255) / 256;
        if (g > 4096) g = 4096;
        rf_step_scalar<<<(unsigned int)g, 256, 0, stream>>>(
            (const unsigned short*)d_in[0], (const unsigned short*)d_in[1], (const unsigned short*)d_in[2],
            (unsigned short*)d_out, n, per_batch > 0 ? per_batch : 1);
    }
}
```

```cpp
#include <hip/hip_runtime.h>
#include <hip/hip_bf16.h>
#include <stdint.h>

typedef unsigned int u32x4 __attribute__((ext_vector_type(4)));
typedef float f32x2 __attribute__((ext_vector_type(2)));
typedef __bf16 bf16x2 __attribute__((ext_vector_type(2)));

#define RF_BLOCK 256
#define RF_UNROLL 4
#define RF_LOAD_AUX 2
#define RF_STORE_AUX 16
#define RF_CHUNK_BYTES (RF_BLOCK * RF_UNROLL * 16)

__device__ __forceinline__ unsigned int rf_pair(unsigned int xw, unsigned int vw, float s) {
    f32x2 xf, vf;
    xf.x = __uint_as_float(xw << 16);
    xf.y = __uint_as_float(xw & 0xffff0000u);
    vf.x = __uint_as_float(vw << 16);
    vf.y = __uint_as_float(vw & 0xffff0000u);
    const f32x2 p = vf * s;
    const unsigned int tw = __builtin_bit_cast(unsigned int, __builtin_convertvector(p, bf16x2));
    f32x2 tf;
    tf.x = __uint_as_float(tw << 16);
    tf.y = __uint_as_float(tw & 0xffff0000u);
    const f32x2 r = xf - tf;
    return __builtin_bit_cast(unsigned int, __builtin_convertvector(r, bf16x2));
}

__global__ __launch_bounds__(RF_BLOCK) void rf_step_vec(const void* __restrict__ x, const void* __restrict__ v,
                                                        void* __restrict__ out,
                                                        const unsigned int* __restrict__ sigma2,
                                                        unsigned int batch_bytes, unsigned int total_bytes) {
    const unsigned int b = blockIdx.y;
    const auto rx = __builtin_amdgcn_make_buffer_rsrc((void*)x, 0, total_bytes, 0x00020000);
    const auto rv = __builtin_amdgcn_make_buffer_rsrc((void*)v, 0, total_bytes, 0x00020000);
    const auto ro = __builtin_amdgcn_make_buffer_rsrc(out, 0, total_bytes, 0x00020000);
    const unsigned int voff = b * batch_bytes + blockIdx.x * RF_CHUNK_BYTES + threadIdx.x * 16;
    u32x4 xa[RF_UNROLL], va[RF_UNROLL];
#pragma unroll
    for (int u = 0; u < RF_UNROLL; ++u) {
        va[u] = __builtin_amdgcn_raw_buffer_load_b128(rv, voff + u * (RF_BLOCK * 16), 0, RF_LOAD_AUX);
        xa[u] = __builtin_amdgcn_raw_buffer_load_b128(rx, voff + u * (RF_BLOCK * 16), 0, RF_LOAD_AUX);
        __builtin_amdgcn_sched_barrier(0);
    }
    const unsigned int sw = sigma2[b >> 1];
    const float s = __uint_as_float((b & 1) ? (sw & 0xffff0000u) : (sw << 16));
    __builtin_amdgcn_sched_barrier(0);
#pragma unroll
    for (int u = 0; u < RF_UNROLL; ++u) {
        u32x4 o;
        o.x = rf_pair(xa[u].x, va[u].x, s);
        o.y = rf_pair(xa[u].y, va[u].y, s);
        o.z = rf_pair(xa[u].z, va[u].z, s);
        o.w = rf_pair(xa[u].w, va[u].w, s);
        __builtin_amdgcn_raw_buffer_store_b128(o, ro, voff + u * (RF_BLOCK * 16), 0, RF_STORE_AUX);
        __builtin_amdgcn_sched_barrier(0);
    }
}

__global__ void rf_step_scalar(const unsigned short* __restrict__ x, const unsigned short* __restrict__ v,
                               const unsigned short* __restrict__ sigma, unsigned short* __restrict__ out,
                               long long n, long long per_batch) {
    long long i = (long long)blockIdx.x * blockDim.x + threadIdx.x;
    const long long stride = (long long)gridDim.x * blockDim.x;
    for (; i < n; i += stride) {
        const float s = __uint_as_float(((unsigned int)sigma[i / per_batch]) << 16);
        out[i] = (unsigned short)(rf_pair(x[i], v[i], s) & 0xffffu);
    }
}

extern "C" void kernel_launch(void* const* d_in, const int* in_sizes, int n_in,
                              void* d_out, int out_size, void* d_ws, size_t ws_size,
                              hipStream_t stream) {
    const long long n = in_sizes[0];
    const long long nb = in_sizes[2] > 0 ? in_sizes[2] : 1;
    const long long per_batch = n / nb;
    const long long chunk = RF_CHUNK_BYTES / 2;
    const bool aligned = (((uintptr_t)d_in[0] | (uintptr_t)d_in[1] | (uintptr_t)d_out) & 15) == 0 &&
                         ((uintptr_t)d_in[2] & 3) == 0;
    if (aligned && per_batch > 0 && per_batch % chunk == 0 && per_batch * nb == n && (nb & 1) == 0 &&
        nb <= 65535 && n * 2 <= 0xffffffffll) {
        dim3 grid((unsigned int)(per_batch / chunk), (unsigned int)nb);
        rf_step_vec<<<grid, RF_BLOCK, 0, stream>>>(d_in[0], d_in[1], d_out, (const unsigned int*)d_in[2],
                                                   (unsigned int)(per_batch * 2), (unsigned int)(n * 2));
    } else if (n > 0) {
        long long g = (n + 255) / 256;
        if (g > 4096) g = 4096;
        rf_step_scalar<<<(unsigned int)g, 256, 0, stream>>>(
            (const unsigned short*)d_in[0], (const unsigned short*)d_in[1], (const unsigned short*)d_in[2],
            (unsigned short*)d_out, n, per_batch > 0 ? per_batch : 1);
    }
}
```

```cpp
#include <hip/hip_runtime.h>
#include <hip/hip_bf16.h>
#include <stdint.h>

typedef unsigned int u32x4 __attribute__((ext_vector_type(4)));
typedef float f32x2 __attribute__((ext_vector_type(2)));
typedef __bf16 bf16x2 __attribute__((ext_vector_type(2)));

#define RF_BLOCK 512
#define RF_UNROLL 10
#define RF_LOAD_AUX 2
#define RF_STORE_AUX 16
#define RF_GATE 7
#define RF_CHUNK_BYTES (RF_BLOCK * RF_UNROLL * 16)

__device__ __forceinline__ unsigned int rf_pair(unsigned int xw, unsigned int vw, float s) {
    f32x2 xf, vf;
    xf.x = __uint_as_float(xw << 16);
    xf.y = __uint_as_float(xw & 0xffff0000u);
    vf.x = __uint_as_float(vw << 16);
    vf.y = __uint_as_float(vw & 0xffff0000u);
    const f32x2 p = vf * s;
    const unsigned int tw = __builtin_bit_cast(unsigned int, __builtin_convertvector(p, bf16x2));
    f32x2 tf;
    tf.x = __uint_as_float(tw << 16);
    tf.y = __uint_as_float(tw & 0xffff0000u);
    const f32x2 r = xf - tf;
    return __builtin_bit_cast(unsigned int, __builtin_convertvector(r, bf16x2));
}

__global__ __launch_bounds__(RF_BLOCK) void rf_step_vec(const void* __restrict__ x, const void* __restrict__ v,
                                                        void* __restrict__ out,
                                                        const unsigned int* __restrict__ sigma2,
                                                        unsigned int batch_bytes, unsigned int total_bytes) {
    const unsigned int b = blockIdx.y;
    const auto rx = __builtin_amdgcn_make_buffer_rsrc((void*)x, 0, total_bytes, 0x00020000);
    const auto rv = __builtin_amdgcn_make_buffer_rsrc((void*)v, 0, total_bytes, 0x00020000);
    const auto ro = __builtin_amdgcn_make_buffer_rsrc(out, 0, total_bytes, 0x00020000);
    const unsigned int voff = b * batch_bytes + blockIdx.x * RF_CHUNK_BYTES + threadIdx.x * 16;
    u32x4 xa[RF_UNROLL], va[RF_UNROLL];
#pragma unroll
    for (int u = 0; u < RF_UNROLL; ++u) {
        va[u] = __builtin_amdgcn_raw_buffer_load_b128(rv, voff + u * (RF_BLOCK * 16), 0, RF_LOAD_AUX);
        xa[u] = __builtin_amdgcn_raw_buffer_load_b128(rx, voff + u * (RF_BLOCK * 16), 0, RF_LOAD_AUX);
        __builtin_amdgcn_sched_barrier(0);
    }
    const unsigned int sw = sigma2[b >> 1];
    const float s = __uint_as_float((b & 1) ? (sw & 0xffff0000u) : (sw << 16));
    __builtin_amdgcn_sched_barrier(0);
#pragma unroll
    for (int u = 0; u < RF_UNROLL; ++u) {
        u32x4 o;
        o.x = rf_pair(xa[u].x, va[u].x, s);
        o.y = rf_pair(xa[u].y, va[u].y, s);
        o.z = rf_pair(xa[u].z, va[u].z, s);
        o.w = rf_pair(xa[u].w, va[u].w, s);
        xa[u] = o;
        __builtin_amdgcn_sched_barrier(0);
        if (u == RF_GATE) {
            __builtin_amdgcn_s_barrier();
            __builtin_amdgcn_sched_barrier(0);
#pragma unroll
            for (int k = 0; k <= RF_GATE; ++k)
                __builtin_amdgcn_raw_buffer_store_b128(xa[k], ro, voff + k * (RF_BLOCK * 16), 0, RF_STORE_AUX);
            __builtin_amdgcn_sched_barrier(0);
        } else if (u > RF_GATE) {
            __builtin_amdgcn_raw_buffer_store_b128(xa[u], ro, voff + u * (RF_BLOCK * 16), 0, RF_STORE_AUX);
            __builtin_amdgcn_sched_barrier(0);
        }
    }
}

__global__ void rf_step_scalar(const unsigned short* __restrict__ x, const unsigned short* __restrict__ v,
                               const unsigned short* __restrict__ sigma, unsigned short* __restrict__ out,
                               long long n, long long per_batch) {
    long long i = (long long)blockIdx.x * blockDim.x + threadIdx.x;
    const long long stride = (long long)gridDim.x * blockDim.x;
    for (; i < n; i += stride) {
        const float s = __uint_as_float(((unsigned int)sigma[i / per_batch]) << 16);
        out[i] = (unsigned short)(rf_pair(x[i], v[i], s) & 0xffffu);
    }
}

extern "C" void kernel_launch(void* const* d_in, const int* in_sizes, int n_in,
                              void* d_out, int out_size, void* d_ws, size_t ws_size,
                              hipStream_t stream) {
    const long long n = in_sizes[0];
    const long long nb = in_sizes[2] > 0 ? in_sizes[2] : 1;
    const long long per_batch = n / nb;
    const long long chunk = RF_CHUNK_BYTES / 2;
    const bool aligned = (((uintptr_t)d_in[0] | (uintptr_t)d_in[1] | (uintptr_t)d_out) & 15) == 0 &&
                         ((uintptr_t)d_in[2] & 3) == 0;
    if (aligned && per_batch > 0 && per_batch % chunk == 0 && per_batch * nb == n && (nb & 1) == 0 &&
        nb <= 65535 && n * 2 <= 0xffffffffll) {
        dim3 grid((unsigned int)(per_batch / chunk), (unsigned int)nb);
        rf_step_vec<<<grid, RF_BLOCK, 0, stream>>>(d_in[0], d_in[1], d_out, (const unsigned int*)d_in[2],
                                                   (unsigned int)(per_batch * 2), (unsigned int)(n * 2));
    } else if (n > 0) {
        long long g = (n + 255) / 256;
        if (g > 4096) g = 4096;
        rf_step_scalar<<<(unsigned int)g, 256, 0, stream>>>(
            (const unsigned short*)d_in[0], (const unsigned short*)d_in[1], (const unsigned short*)d_in[2],
            (unsigned short*)d_out, n, per_batch > 0 ? per_batch : 1);
    }
}
```

```cpp
#include <hip/hip_runtime.h>
#include <hip/hip_bf16.h>
#include <stdint.h>

typedef unsigned int u32x4 __attribute__((ext_vector_type(4)));
typedef float f32x2 __attribute__((ext_vector_type(2)));
typedef __bf16 bf16x2 __attribute__((ext_vector_type(2)));

#define RF_BLOCK 512
#define RF_UNROLL 10
#define RF_GATE 8
#define RF_AUX_NT 2
#define RF_STEP_BYTES (RF_BLOCK * 16)
#define RF_CHUNK_BYTES (RF_STEP_BYTES * RF_UNROLL)

__device__ __forceinline__ unsigned int rf_pair(unsigned int xw, unsigned int vw, float s) {
    f32x2 xf, vf;
    xf.x = __uint_as_float(xw << 16);
    xf.y = __uint_as_float(xw & 0xffff0000u);
    vf.x = __uint_as_float(vw << 16);
    vf.y = __uint_as_float(vw & 0xffff0000u);
    const f32x2 p = vf * s;
    const unsigned int tw = __builtin_bit_cast(unsigned int, __builtin_convertvector(p, bf16x2));
    f32x2 tf;
    tf.x = __uint_as_float(tw << 16);
    tf.y = __uint_as_float(tw & 0xffff0000u);
    const f32x2 r = xf - tf;
    return __builtin_bit_cast(unsigned int, __builtin_convertvector(r, bf16x2));
}

__device__ __forceinline__ u32x4 rf_vec8(u32x4 x, u32x4 v, float s) {
    u32x4 o;
    o.x = rf_pair(x.x, v.x, s);
    o.y = rf_pair(x.y, v.y, s);
    o.z = rf_pair(x.z, v.z, s);
    o.w = rf_pair(x.w, v.w, s);
    return o;
}

__global__ __launch_bounds__(RF_BLOCK) void rf_step_main(const void* __restrict__ x, const void* __restrict__ v,
                                                         void* __restrict__ out,
                                                         const unsigned int* __restrict__ sigma2,
                                                         unsigned int batch_bytes, unsigned int total_bytes) {
    const unsigned int b = blockIdx.y;
    const auto rx = __builtin_amdgcn_make_buffer_rsrc((void*)x, 0, total_bytes, 0x00020000);
    const auto rv = __builtin_amdgcn_make_buffer_rsrc((void*)v, 0, total_bytes, 0x00020000);
    const auto ro = __builtin_amdgcn_make_buffer_rsrc(out, 0, total_bytes, 0x00020000);
    const unsigned int voff = b * batch_bytes + blockIdx.x * RF_CHUNK_BYTES + threadIdx.x * 16;
    u32x4 xa[RF_UNROLL], va[RF_UNROLL];
#pragma unroll
    for (int u = 0; u < RF_UNROLL; ++u) {
        va[u] = __builtin_amdgcn_raw_buffer_load_b128(rv, voff + u * RF_STEP_BYTES, 0, RF_AUX_NT);
        xa[u] = __builtin_amdgcn_raw_buffer_load_b128(rx, voff + u * RF_STEP_BYTES, 0, RF_AUX_NT);
        __builtin_amdgcn_sched_barrier(0);
    }
    const unsigned int sw = sigma2[b >> 1];
    const float s = __uint_as_float((b & 1) ? (sw & 0xffff0000u) : (sw << 16));
    __builtin_amdgcn_sched_barrier(0);
#pragma unroll
    for (int u = 0; u < RF_UNROLL; ++u) {
        xa[u] = rf_vec8(xa[u], va[u], s);
        __builtin_amdgcn_sched_barrier(0);
        if (u == RF_GATE) {
            __builtin_amdgcn_s_barrier();
            __builtin_amdgcn_sched_barrier(0);
#pragma unroll
            for (int k = 0; k <= RF_GATE; ++k)
                __builtin_amdgcn_raw_buffer_store_b128(xa[k], ro, voff + k * RF_STEP_BYTES, 0, RF_AUX_NT);
            __builtin_amdgcn_sched_barrier(0);
        } else if (u > RF_GATE) {
            __builtin_amdgcn_raw_buffer_store_b128(xa[u], ro, voff + u * RF_STEP_BYTES, 0, RF_AUX_NT);
            __builtin_amdgcn_sched_barrier(0);
        }
    }
}

__global__ __launch_bounds__(256) void rf_step_vec8(const u32x4* __restrict__ x, const u32x4* __restrict__ v,
                                                    const unsigned short* __restrict__ sigma,
                                                    u32x4* __restrict__ out, unsigned int nvec,
                                                    unsigned int vec_per_batch) {
    const unsigned int stride = gridDim.x * 256;
    for (unsigned int i = blockIdx.x * 256 + threadIdx.x; i < nvec; i += stride) {
        const float s = __uint_as_float(((unsigned int)sigma[i / vec_per_batch]) << 16);
        out[i] = rf_vec8(x[i], v[i], s);
    }
}

__global__ __launch_bounds__(256) void rf_step_scalar(const unsigned short* __restrict__ x,
                                                      const unsigned short* __restrict__ v,
                                                      const unsigned short* __restrict__ sigma,
                                                      unsigned short* __restrict__ out, unsigned int n,
                                                      unsigned int per_batch) {
    const unsigned int stride = gridDim.x * 256;
    for (unsigned int i = blockIdx.x * 256 + threadIdx.x; i < n; i += stride) {
        const float s = __uint_as_float(((unsigned int)sigma[i / per_batch]) << 16);
        out[i] = (unsigned short)(rf_pair(x[i], v[i], s) & 0xffffu);
    }
}

extern "C" void kernel_launch(void* const* d_in, const int* in_sizes, int n_in,
                              void* d_out, int out_size, void* d_ws, size_t ws_size,
                              hipStream_t stream) {
    const long long n = in_sizes[0];
    const long long nb = in_sizes[2] > 0 ? in_sizes[2] : 1;
    const long long per_batch = n / nb;
    if (n <= 0 || per_batch <= 0) return;
    const long long chunk = RF_CHUNK_BYTES / 2;
    const bool aligned = (((uintptr_t)d_in[0] | (uintptr_t)d_in[1] | (uintptr_t)d_out) & 15) == 0;
    if (aligned && ((uintptr_t)d_in[2] & 3) == 0 && (nb & 1) == 0 && nb <= 65535 && per_batch % chunk == 0) {
        dim3 grid((unsigned int)(per_batch / chunk), (unsigned int)nb);
        rf_step_main<<<grid, RF_BLOCK, 0, stream>>>(d_in[0], d_in[1], d_out, (const unsigned int*)d_in[2],
                                                    (unsigned int)(per_batch * 2), (unsigned int)(n * 2));
    } else if (aligned && per_batch % 8 == 0) {
        const long long nvec = n / 8;
        long long g = (nvec + 255) / 256;
        if (g > 2048) g = 2048;
        rf_step_vec8<<<(unsigned int)g, 256, 0, stream>>>((const u32x4*)d_in[0], (const u32x4*)d_in[1],
                                                          (const unsigned short*)d_in[2], (u32x4*)d_out,
                                                          (unsigned int)nvec, (unsigned int)(per_batch / 8));
    } else {
        long long g = (n + 255) / 256;
        if (g > 4096) g = 4096;
        rf_step_scalar<<<(unsigned int)g, 256, 0, stream>>>(
            (const unsigned short*)d_in[0], (const unsigned short*)d_in[1], (const unsigned short*)d_in[2],
            (unsigned short*)d_out, (unsigned int)n, (unsigned int)per_batch);
    }
}
```
